```python
import torch

def _softmax(x, axis=-1):
    return torch.softmax(x, dim=axis)
def _gelu(x):
    return torch.nn.functional.gelu(x, approximate="tanh")
def _silu(x):
    return torch.nn.functional.silu(x)
def _sigmoid(x):
    return torch.sigmoid(x)
def _layernorm(x, axis=-1, eps=1e-5):
    m = x.mean(dim=axis, keepdim=True)
    v = x.var(dim=axis, keepdim=True, unbiased=False)
    return (x - m) / torch.sqrt(v + eps)
def _scatter_add(tgt, idx, src, axis=0):
    return tgt.clone().index_add_(axis, idx, src)
def _segment_sum(src, seg, n):
    out = torch.zeros((n,) + src.shape[1:], dtype=src.dtype)
    return out.index_add_(0, seg, src)
def _pad(x, pw):
    flat = tuple(v for pair in reversed(pw) for v in pair)
    return torch.nn.functional.pad(x, flat)

kernel_name = 'standalone__self_sup_conv_stem_hubert__speech'

def setup_inputs():

    _w = (lambda a: (lambda t: t.to(torch.bfloat16) if t.is_floating_point() else t)(torch.as_tensor(a)))
    B, T, D, K = 2, 16000, 512, 5
    scale = (1.0 / (K * D)) ** 0.5
    return {
        "x": _w((lambda *s: torch.randn(*s, dtype=torch.bfloat16) * 0.05)(B, T, D)),
        "w": _w((lambda *s: torch.randn(*s, dtype=torch.bfloat16) * 0.05)(K, D, D) * scale),
        "b": _w((lambda *s: torch.randn(*s, dtype=torch.bfloat16) * 0.05)(D) * 0.1),
        "gamma": _w(torch.ones((D,), dtype=float)),
        "beta": _w(torch.zeros((D,), dtype=float)),
    }

def reference(x, w, b, gamma, beta):
    B, T, D = x.shape
    K = w.shape[0]
    s = 2
    L = (T - K) // s + 1
    y = torch.einsum('blc,cd->bld', x[:, 0 : s * L : s, :], w[0])
    for i in range(1, K):
        y = y + torch.einsum('blc,cd->bld', x[:, i : i + s * L : s, :], w[i])
    y = y + b[None, None, :]
    mu = y.mean(1, keepdims=True)
    var = ((y - mu) ** 2).mean(1, keepdims=True)
    yn = (y - mu) / torch.sqrt(var + 1e-5)
    yn = yn * gamma[None, None, :] + beta[None, None, :]
    return _gelu(yn)
```

```cpp
#include <hip/hip_runtime.h>
#include <hip/hip_bf16.h>

typedef __attribute__((ext_vector_type(8))) short bf16x8;
typedef __attribute__((ext_vector_type(4))) float f32x4;
typedef __attribute__((ext_vector_type(4))) unsigned u32x4;

constexpr int T_ = 16000, D_ = 512, L_ = 7998;
constexpr int BM = 256, BN = 128, MTB = 32, NPH = 80;
constexpr int AUNIT = 272 * 64;
constexpr int BSLOT = 8192;
constexpr int BOFF = 5 * AUNIT;
constexpr int LDS_BYTES = BOFF + 6 * BSLOT;

constexpr size_t WS_WP = 0;
constexpr size_t WS_STATS = 4u << 20;
constexpr size_t WS_CNT = 6u << 20;

__device__ __forceinline__ int perm32(int rho) {
    const int n = rho >> 4, i = rho & 15;
    return 8 * (i >> 2) + 4 * n + (i & 3);
}

__global__ __launch_bounds__(256) void prep_kernel(const unsigned short* __restrict__ w,
                                                   unsigned short* __restrict__ wp,
                                                   unsigned* __restrict__ cnt) {
    __shared__ unsigned short tile[32][136];
    const int blk = blockIdx.x;
    const int nt = blk / NPH, ph = blk % NPH;
    const int c = ph / 5, i = ph % 5;
    const int tap = (i < 3) ? 2 * i : 2 * (i - 3) + 1;
    const int tid = threadIdx.x;
    if (blk == 0 && tid < 8) cnt[tid * 64] = 0u;
#pragma unroll
    for (int k = 0; k < 2; ++k) {
        const int idx = tid + k * 256;
        const int r = idx >> 4, cc = idx & 15;
        const u32x4 v = *(const u32x4*)(w + ((size_t)tap * 512 + c * 32 + r) * 512 + nt * 128 + cc * 8);
        *(u32x4*)(&tile[r][cc * 8]) = v;
    }
    __syncthreads();
#pragma unroll
    for (int k = 0; k < 2; ++k) {
        const int idx = tid + k * 256;
        const int rho = idx >> 2, chs = idx & 3;
        const int ch = chs ^ (((rho >> 2) & 1) << 1);
        const int col = (rho & ~31) + perm32(rho & 31);
        unsigned short e[8];
#pragma unroll
        for (int j = 0; j < 8; ++j) e[j] = tile[ch * 8 + j][col];
        u32x4 v;
        v.x = e[0] | ((unsigned)e[1] << 16);
        v.y = e[2] | ((unsigned)e[3] << 16);
        v.z = e[4] | ((unsigned)e[5] << 16);
        v.w = e[6] | ((unsigned)e[7] << 16);
        *(u32x4*)(wp + (size_t)blk * 4096 + idx * 8) = v;
    }
}

#define WAIT_V(n) asm volatile("s_waitcnt vmcnt(" #n ")" ::: "memory")
#define WAIT_L0 asm volatile("s_waitcnt lgkmcnt(0)" ::: "memory")
#define BAR __builtin_amdgcn_s_barrier()
#define GLDS(g, l) __builtin_amdgcn_global_load_lds((const unsigned*)(g), (unsigned*)(l), 16, 0, 0)

__device__ __forceinline__ float bf2f(unsigned short u) { return __uint_as_float(((unsigned)u) << 16); }
__device__ __forceinline__ unsigned pack_bf16(float lo, float hi) {
    __hip_bfloat16 a = __float2bfloat16(lo), b = __float2bfloat16(hi);
    return (unsigned)(*(unsigned short*)&a) | ((unsigned)(*(unsigned short*)&b) << 16);
}
__device__ __forceinline__ float gelu_tanh(float x) {
    const float c0 = -2.0f * 0.7978845608028654f * 1.4426950408889634f;
    const float c1 = c0 * 0.044715f;
    const float p = fmaf(x * x, c1, c0);
    const float e = __builtin_amdgcn_exp2f(x * p);
    return x * __builtin_amdgcn_rcpf(1.0f + e);
}

__global__ __launch_bounds__(512, 2) void conv_gn_kernel(
    const unsigned short* __restrict__ x, const unsigned short* __restrict__ wp,
    const unsigned short* __restrict__ bias, const unsigned short* __restrict__ gamma,
    const unsigned short* __restrict__ beta, unsigned short* __restrict__ out,
    float* __restrict__ stats, unsigned* __restrict__ cnt) {
    extern __shared__ __attribute__((aligned(16))) char smem[];
    const int tid = threadIdx.x;
    const int wid = __builtin_amdgcn_readfirstlane(tid >> 6);
    const int lane = tid & 63;
    const int fr = lane & 15, fq = lane >> 4;
    const int wm = wid >> 1, wn = wid & 1;
    const int bid = blockIdx.x;
    const int g = bid & 7, jj = bid >> 3;
    const int mtg = g * 8 + (jj >> 2);
    const int ntile = jj & 3;
    const int b = mtg >> 5, mtile = mtg & 31;
    const int l0 = mtile * BM;

    const int prow = lane >> 2, pch = lane & 3;
    const int pswz = pch ^ (((prow >> 2) & 1) << 1);
    unsigned aoff[4];
#pragma unroll
    for (int k = 0; k < 4; ++k) {
        int t = 2 * (l0 + (k & 1) * 128 + wid * 16 + prow) + (k >> 1);
        t = t < T_ - 1 ? t : T_ - 1;
        aoff[k] = (unsigned)((b * T_ + t) * 1024 + pswz * 16);
    }
    unsigned aoffx;
    {
        int t = 2 * (l0 + 256 + prow) + (wid & 1);
        t = t < T_ - 1 ? t : T_ - 1;
        aoffx = (unsigned)((b * T_ + t) * 1024 + pswz * 16);
    }
    const char* xb = (const char*)x;
    const char* wb = (const char*)wp + (size_t)ntile * NPH * BSLOT + wid * 1024 + lane * 16;

    unsigned aro[3];
#pragma unroll
    for (int s = 0; s < 3; ++s) {
        const int r = wm * 64 + fr + s;
        aro[s] = (unsigned)(r * 64 + ((fq ^ (((r >> 2) & 1) << 1)) * 16));
    }
    unsigned bro;
    {
        const int r = wn * 64 + fr;
        bro = (unsigned)(BOFF + r * 64 + ((fq ^ (((r >> 2) & 1) << 1)) * 16));
    }

    f32x4 acc[4][4];
#pragma unroll
    for (int m = 0; m < 4; ++m)
#pragma unroll
        for (int n = 0; n < 4; ++n) acc[m][n] = (f32x4){0.f, 0.f, 0.f, 0.f};
    bf16x8 ra[2][4], rb[2][4];

#pragma unroll
    for (int u = 0; u < 4; ++u) {
        const int c = u >> 1, eo = u & 1;
#pragma unroll
        for (int h = 0; h < 2; ++h)
            GLDS(xb + aoff[eo * 2 + h] + c * 64, smem + u * AUNIT + h * 8192 + wid * 1024 + lane * 16);
        if (wid == eo) GLDS(xb + aoffx + c * 64, smem + u * AUNIT + 16384 + lane * 16);
    }
#pragma unroll
    for (int p = 0; p < 5; ++p) GLDS(wb + p * BSLOT, smem + BOFF + p * BSLOT + wid * 1024 + lane * 16);
    WAIT_V(0);
    BAR;

#define READ_FRAGS(R, ua, sh, sb)                                                               \
    do {                                                                                        \
        const char* ap_ = smem + (ua) * AUNIT + aro[sh];                                        \
        const char* bp_ = smem + (sb) * BSLOT + bro;                                            \
        _Pragma("unroll") for (int m = 0; m < 4; ++m) ra[R][m] = *(const bf16x8*)(ap_ + m * 1024); \
        _Pragma("unroll") for (int n = 0; n < 4; ++n) rb[R][n] = *(const bf16x8*)(bp_ + n * 1024); \
    } while (0)
#define MMA(R)                                                                                  \
    do {                                                                                        \
        __builtin_amdgcn_s_setprio(1);                                                          \
        _Pragma("unroll") for (int m = 0; m < 4; ++m) _Pragma("unroll") for (int n = 0; n < 4; ++n) \
            acc[m][n] = __builtin_amdgcn_mfma_f32_16x16x32_bf16(rb[R][n], ra[R][m], acc[m][n], 0, 0, 0); \
        __builtin_amdgcn_s_setprio(0);                                                          \
    } while (0)

    READ_FRAGS(0, 0, 0, 0);

    int ue = 0;
    int sbn = 0;

#define PHASE(I, R, NV, LAST)                                                                   \
    do {                                                                                        \
        WAIT_L0;                                                                                \
        WAIT_V(NV);                                                                             \
        BAR;                                                                                    \
        if (!(LAST && I == 4)) {                                                                \
            const int un_ = (I == 4) ? (ue + 2) % 5 : ((I >= 2) ? (ue + 1) % 5 : ue);           \
            const int sh_ = (I == 4) ? 0 : ((I >= 2) ? I - 2 : I + 1);                          \
            const int sb_ = (sbn + 1) % 6;                                                      \
            READ_FRAGS(R ^ 1, un_, sh_, sb_);                                                   \
        }                                                                                       \
        if (!(LAST)) {                                                                          \
            if (pf) {                                                                           \
                if (I == 0 || I == 1)                                                           \
                    GLDS(xb + aoff[I] + (c + 2) * 64,                                           \
                         smem + ((ue + 4) % 5) * AUNIT + I * 8192 + wid * 1024 + lane * 16);    \
                if (I == 3 || I == 4)                                                           \
                    GLDS(xb + aoff[I - 1] + (c + 2) * 64,                                       \
                         smem + ue * AUNIT + (I - 3) * 8192 + wid * 1024 + lane * 16);          \
                if (I == 2 && wid < 2)                                                          \
                    GLDS(xb + aoffx + (c + 2) * 64,                                             \
                         smem + ((wid == 0) ? ((ue + 4) % 5) : ue) * AUNIT + 16384 + lane * 16); \
            }                                                                                   \
            GLDS(wb + (c * 5 + I + 5) * BSLOT,                                                  \
                 smem + BOFF + ((sbn + 5) % 6) * BSLOT + wid * 1024 + lane * 16);               \
        }                                                                                       \
        MMA(R);                                                                                 \
        sbn = (sbn + 1) % 6;                                                                    \
    } while (0)

    for (int c = 0; c < 14; c += 2) {
        {
            const bool pf = true;
            PHASE(0, 0, 5, false); PHASE(1, 1, 6, false); PHASE(2, 0, 6, false); PHASE(3, 1, 5, false); PHASE(4, 0, 5, false);
            ue = (ue + 2) % 5;
        }
        {
            ++c;
            const bool pf = true;
            PHASE(0, 1, 5, false); PHASE(1, 0, 6, false); PHASE(2, 1, 6, false); PHASE(3, 0, 5, false); PHASE(4, 1, 5, false);
            ue = (ue + 2) % 5;
            --c;
        }
    }
    {
        const int c = 14;
        const bool pf = false;
        PHASE(0, 0, 3, false); PHASE(1, 1, 3, false); PHASE(2, 0, 3, false); PHASE(3, 1, 3, false); PHASE(4, 0, 3, false);
        ue = (ue + 2) % 5;
    }
    {
        const int c = 15;
        const bool pf = false;
        PHASE(0, 1, 3, true); PHASE(1, 0, 2, true); PHASE(2, 1, 1, true); PHASE(3, 0, 0, true); PHASE(4, 1, 0, true);
    }
    WAIT_L0;
    WAIT_V(0);
    BAR;

    float* red = (float*)smem;
    float* red2 = red + 512;
    float* csc = red2 + 512;
    float* csh = csc + 128;
    float* cmb = csh + 128;

    float bv[4][4];
#pragma unroll
    for (int n = 0; n < 4; ++n)
#pragma unroll
        for (int r = 0; r < 4; ++r)
            bv[n][r] = bf2f(bias[ntile * 128 + wn * 64 + (n >> 1) * 32 + 8 * fq + 4 * (n & 1) + r]);
    bool valid[4];
#pragma unroll
    for (int m = 0; m < 4; ++m) valid[m] = (l0 + wm * 64 + m * 16 + fr) < L_;
#pragma unroll
    for (int m = 0; m < 4; ++m)
#pragma unroll
        for (int n = 0; n < 4; ++n)
#pragma unroll
            for (int r = 0; r < 4; ++r) acc[m][n][r] += bv[n][r];

    const int cnt_rows = (L_ - l0) < BM ? (L_ - l0) : BM;
    float mean_t[4][4];
#pragma unroll
    for (int n = 0; n < 4; ++n)
#pragma unroll
        for (int r = 0; r < 4; ++r) {
            float s = 0.f;
#pragma unroll
            for (int m = 0; m < 4; ++m) s += valid[m] ? acc[m][n][r] : 0.f;
            s += __shfl_xor(s, 1); s += __shfl_xor(s, 2); s += __shfl_xor(s, 4); s += __shfl_xor(s, 8);
            if (fr == 0) red[wm * 128 + wn * 64 + (n >> 1) * 32 + 8 * fq + 4 * (n & 1) + r] = s;
        }
    __syncthreads();
    const float inv_cnt = 1.0f / (float)cnt_rows;
#pragma unroll
    for (int n = 0; n < 4; ++n)
#pragma unroll
        for (int r = 0; r < 4; ++r) {
            const int col = wn * 64 + (n >> 1) * 32 + 8 * fq + 4 * (n & 1) + r;
            mean_t[n][r] = (red[col] + red[128 + col] + red[256 + col] + red[384 + col]) * inv_cnt;
        }
#pragma unroll
    for (int n = 0; n < 4; ++n)
#pragma unroll
        for (int r = 0; r < 4; ++r) {
            float s = 0.f;
#pragma unroll
            for (int m = 0; m < 4; ++m) {
                const float d = acc[m][n][r] - mean_t[n][r];
                s += valid[m] ? d * d : 0.f;
            }
            s += __shfl_xor(s, 1); s += __shfl_xor(s, 2); s += __shfl_xor(s, 4); s += __shfl_xor(s, 8);
            if (fr == 0) red2[wm * 128 + wn * 64 + (n >> 1) * 32 + 8 * fq + 4 * (n & 1) + r] = s;
        }
    __syncthreads();
    if (tid < 128) {
        const float mt = (red[tid] + red[128 + tid] + red[256 + tid] + red[384 + tid]) * inv_cnt;
        const float m2 = red2[tid] + red2[128 + tid] + red2[256 + tid] + red2[384 + tid];
        float2 v; v.x = mt; v.y = m2;
        *(float2*)(stats + ((size_t)mtg * 512 + ntile * 128 + tid) * 2) = v;
    }
    asm volatile("s_waitcnt vmcnt(0)" ::: "memory");
    __syncthreads();
    if (tid == 0) {
        unsigned* cp = cnt + (b * 4 + ntile) * 64;
        __builtin_amdgcn_fence(__ATOMIC_RELEASE, "agent");
        asm volatile("s_waitcnt vmcnt(0)" ::: "memory");
        __hip_atomic_fetch_add(cp, 1u, __ATOMIC_RELAXED, __HIP_MEMORY_SCOPE_AGENT);
        unsigned spins = 0;
        while (__hip_atomic_load(cp, __ATOMIC_RELAXED, __HIP_MEMORY_SCOPE_AGENT) < (unsigned)MTB) {
            __builtin_amdgcn_s_sleep(1);
            if (++spins > (1u << 24)) break;
        }
        __builtin_amdgcn_fence(__ATOMIC_ACQUIRE, "agent");
        asm volatile("s_waitcnt vmcnt(0)" ::: "memory");
    }
    __syncthreads();
    {
        const int col = tid & 127, part = tid >> 7;
        float mi[8], m2i[8], ni[8];
        float s = 0.f;
#pragma unroll
        for (int k = 0; k < 8; ++k) {
            const int tt = part * 8 + k;
            const float2 v = *(const float2*)(stats + ((size_t)(b * 32 + tt) * 512 + ntile * 128 + col) * 2);
            mi[k] = v.x; m2i[k] = v.y;
            ni[k] = (tt == 31) ? (float)(L_ - 31 * BM) : (float)BM;
            s += ni[k] * mi[k];
        }
        cmb[part * 128 + col] = s;
        __syncthreads();
        const float mean = (cmb[col] + cmb[128 + col] + cmb[256 + col] + cmb[384 + col]) * (1.0f / (float)L_);
        float q = 0.f;
#pragma unroll
        for (int k = 0; k < 8; ++k) { const float d = mi[k] - mean; q += m2i[k] + ni[k] * d * d; }
        __syncthreads();
        cmb[part * 128 + col] = q;
        __syncthreads();
        if (tid < 128) {
            const float var = (cmb[col] + cmb[128 + col] + cmb[256 + col] + cmb[384 + col]) * (1.0f / (float)L_);
            const float rstd = 1.0f / sqrtf(var + 1e-5f);
            const float gm = bf2f(gamma[ntile * 128 + col]), bt = bf2f(beta[ntile * 128 + col]);
            csc[col] = rstd * gm;
            csh[col] = bt - mean * rstd * gm;
        }
        __syncthreads();
    }
    float sc[4][4], sh[4][4];
#pragma unroll
    for (int n = 0; n < 4; ++n)
#pragma unroll
        for (int r = 0; r < 4; ++r) {
            const int col = wn * 64 + (n >> 1) * 32 + 8 * fq + 4 * (n & 1) + r;
            sc[n][r] = csc[col]; sh[n][r] = csh[col];
        }
#pragma unroll
    for (int m = 0; m < 4; ++m) {
        const int l = l0 + wm * 64 + m * 16 + fr;
        if (l < L_) {
            unsigned short* orow = out + ((size_t)(b * L_ + l)) * 512 + ntile * 128 + wn * 64 + 8 * fq;
#pragma unroll
            for (int pr = 0; pr < 2; ++pr) {
                float v[8];
#pragma unroll
                for (int h = 0; h < 2; ++h)
#pragma unroll
                    for (int r = 0; r < 4; ++r)
                        v[h * 4 + r] = gelu_tanh(fmaf(acc[m][pr * 2 + h][r], sc[pr * 2 + h][r], sh[pr * 2 + h][r]));
                u32x4 o;
                o.x = pack_bf16(v[0], v[1]); o.y = pack_bf16(v[2], v[3]);
                o.z = pack_bf16(v[4], v[5]); o.w = pack_bf16(v[6], v[7]);
                *(u32x4*)(orow + pr * 32) = o;
            }
        }
    }
}

extern "C" void kernel_launch(void* const* d_in, const int* in_sizes, int n_in,
                              void* d_out, int out_size, void* d_ws, size_t ws_size,
                              hipStream_t stream) {
    const unsigned short* x = (const unsigned short*)d_in[0];
    const unsigned short* w = (const unsigned short*)d_in[1];
    const unsigned short* bias = (const unsigned short*)d_in[2];
    const unsigned short* gamma = (const unsigned short*)d_in[3];
    const unsigned short* beta = (const unsigned short*)d_in[4];
    char* ws = (char*)d_ws;
    unsigned short* wp = (unsigned short*)(ws + WS_WP);
    float* stats = (float*)(ws + WS_STATS);
    unsigned* cnt = (unsigned*)(ws + WS_CNT);
    (void)hipFuncSetAttribute((const void*)conv_gn_kernel, hipFuncAttributeMaxDynamicSharedMemorySize, LDS_BYTES);
    prep_kernel<<<4 * NPH, 256, 0, stream>>>(w, wp, cnt);
    conv_gn_kernel<<<256, 512, LDS_BYTES, stream>>>(x, wp, bias, gamma, beta, (unsigned short*)d_out, stats, cnt);
}
```

```cpp
#include <hip/hip_runtime.h>
#include <hip/hip_bf16.h>

#define ABL_NOLDS 0
#define ABL_NOEPI 0
#define ABL_NOA 0
#define ABL_NOB 0
#define ABL_NOBAR 0
typedef __attribute__((ext_vector_type(8))) short bf16x8;
typedef __attribute__((ext_vector_type(4))) float f32x4;
typedef __attribute__((ext_vector_type(4))) unsigned u32x4;

constexpr int T_ = 16000, D_ = 512, L_ = 7998;
constexpr int BM = 256, BN = 128, MTB = 32, NPH = 80;
constexpr int AUNIT = 272 * 64;
constexpr int BSLOT = 8192;
constexpr int BOFF = 5 * AUNIT;
constexpr int LDS_BYTES = BOFF + 6 * BSLOT;

constexpr size_t WS_WP = 0;
constexpr size_t WS_STATS = 4u << 20;
constexpr size_t WS_CNT = 6u << 20;

__device__ __forceinline__ int perm32(int rho) {
    const int n = rho >> 4, i = rho & 15;
    return 8 * (i >> 2) + 4 * n + (i & 3);
}

__global__ __launch_bounds__(256) void prep_kernel(const unsigned short* __restrict__ w,
                                                   unsigned short* __restrict__ wp,
                                                   unsigned* __restrict__ cnt) {
    __shared__ unsigned short tile[32][136];
    const int blk = blockIdx.x;
    const int nt = blk / NPH, ph = blk % NPH;
    const int c = ph / 5, i = ph % 5;
    const int tap = (i < 3) ? 2 * i : 2 * (i - 3) + 1;
    const int tid = threadIdx.x;
    if (blk == 0 && tid < 8) cnt[tid * 64] = 0u;
#pragma unroll
    for (int k = 0; k < 2; ++k) {
        const int idx = tid + k * 256;
        const int r = idx >> 4, cc = idx & 15;
        const u32x4 v = *(const u32x4*)(w + ((size_t)tap * 512 + c * 32 + r) * 512 + nt * 128 + cc * 8);
        *(u32x4*)(&tile[r][cc * 8]) = v;
    }
    __syncthreads();
#pragma unroll
    for (int k = 0; k < 2; ++k) {
        const int idx = tid + k * 256;
        const int rho = idx >> 2, chs = idx & 3;
        const int ch = chs ^ (((rho >> 2) & 1) << 1);
        const int col = (rho & ~31) + perm32(rho & 31);
        unsigned short e[8];
#pragma unroll
        for (int j = 0; j < 8; ++j) e[j] = tile[ch * 8 + j][col];
        u32x4 v;
        v.x = e[0] | ((unsigned)e[1] << 16);
        v.y = e[2] | ((unsigned)e[3] << 16);
        v.z = e[4] | ((unsigned)e[5] << 16);
        v.w = e[6] | ((unsigned)e[7] << 16);
        *(u32x4*)(wp + (size_t)blk * 4096 + idx * 8) = v;
    }
}

#define WAIT_V(n) asm volatile("s_waitcnt vmcnt(" #n ")" ::: "memory")
#define WAIT_L0 do { __builtin_amdgcn_s_waitcnt(0xC07F); asm volatile("" ::: "memory"); } while (0)
#define BAR __builtin_amdgcn_s_barrier()
#define GLDS(g, l) __builtin_amdgcn_global_load_lds((const unsigned*)(g), (unsigned*)(l), 16, 0, 0)

__device__ __forceinline__ float bf2f(unsigned short u) { return __uint_as_float(((unsigned)u) << 16); }
__device__ __forceinline__ unsigned pack_bf16(float lo, float hi) {
    __hip_bfloat16 a = __float2bfloat16(lo), b = __float2bfloat16(hi);
    return (unsigned)(*(unsigned short*)&a) | ((unsigned)(*(unsigned short*)&b) << 16);
}
__device__ __forceinline__ float row16_sum(float s) {
    s += __int_as_float(__builtin_amdgcn_update_dpp(0, __float_as_int(s), 0x128, 0xf, 0xf, false));
    s += __int_as_float(__builtin_amdgcn_update_dpp(0, __float_as_int(s), 0x124, 0xf, 0xf, false));
    s += __int_as_float(__builtin_amdgcn_update_dpp(0, __float_as_int(s), 0x122, 0xf, 0xf, false));
    s += __int_as_float(__builtin_amdgcn_update_dpp(0, __float_as_int(s), 0x121, 0xf, 0xf, false));
    return s;
}
__device__ __forceinline__ float gelu_tanh(float x) {
    const float c0 = -2.0f * 0.7978845608028654f * 1.4426950408889634f;
    const float c1 = c0 * 0.044715f;
    const float p = fmaf(x * x, c1, c0);
    const float e = __builtin_amdgcn_exp2f(x * p);
    return x * __builtin_amdgcn_rcpf(1.0f + e);
}

__global__ __launch_bounds__(512, 2) void conv_gn_kernel(
    const unsigned short* __restrict__ x, const unsigned short* __restrict__ wp,
    const unsigned short* __restrict__ bias, const unsigned short* __restrict__ gamma,
    const unsigned short* __restrict__ beta, unsigned short* __restrict__ out,
    float* __restrict__ stats, unsigned* __restrict__ cnt) {
    extern __shared__ __attribute__((aligned(16))) char smem[];
    const int tid = threadIdx.x;
    const int wid = __builtin_amdgcn_readfirstlane(tid >> 6);
    const int lane = tid & 63;
    const int fr = lane & 15, fq = lane >> 4;
    const int wm = wid >> 1, wn = wid & 1;
    const int bid = blockIdx.x;
    const int g = bid & 7, jj = bid >> 3;
    const int mtg = g * 8 + (jj >> 2);
    const int ntile = jj & 3;
    const int b = mtg >> 5, mtile = mtg & 31;
    const int l0 = mtile * BM;

    const int prow = lane >> 2, pch = lane & 3;
    const int pswz = pch ^ (((prow >> 2) & 1) << 1);
    unsigned aoff[4];
#pragma unroll
    for (int k = 0; k < 4; ++k) {
        int t = 2 * (l0 + (k & 1) * 128 + wid * 16 + prow) + (k >> 1);
        t = t < T_ - 1 ? t : T_ - 1;
        aoff[k] = (unsigned)((b * T_ + t) * 1024 + pswz * 16);
    }
    unsigned aoffx;
    {
        int t = 2 * (l0 + 256 + prow) + (wid & 1);
        t = t < T_ - 1 ? t : T_ - 1;
        aoffx = (unsigned)((b * T_ + t) * 1024 + pswz * 16);
    }
    const char* xb = (const char*)x;
    const char* wb = (const char*)wp + (size_t)ntile * NPH * BSLOT + wid * 1024 + lane * 16;

    unsigned aro[3];
#pragma unroll
    for (int s = 0; s < 3; ++s) {
        const int r = wm * 64 + fr + s;
        aro[s] = (unsigned)(r * 64 + ((fq ^ (((r >> 2) & 1) << 1)) * 16));
    }
    unsigned bro;
    {
        const int r = wn * 64 + fr;
        bro = (unsigned)(BOFF + r * 64 + ((fq ^ (((r >> 2) & 1) << 1)) * 16));
    }

    f32x4 acc[4][4];
#pragma unroll
    for (int m = 0; m < 4; ++m)
#pragma unroll
        for (int n = 0; n < 4; ++n) acc[m][n] = (f32x4){0.f, 0.f, 0.f, 0.f};
    bf16x8 ra[2][4], rb[2][4];

#pragma unroll
    for (int u = 0; u < 4; ++u) {
        const int c = u >> 1, eo = u & 1;
#pragma unroll
        for (int h = 0; h < 2; ++h)
            GLDS(xb + aoff[eo * 2 + h] + c * 64, smem + u * AUNIT + h * 8192 + wid * 1024 + lane * 16);
        if (wid == eo) GLDS(xb + aoffx + c * 64, smem + u * AUNIT + 16384 + lane * 16);
    }
#pragma unroll
    for (int p = 0; p < 5; ++p) GLDS(wb + p * BSLOT, smem + BOFF + p * BSLOT + wid * 1024 + lane * 16);
    WAIT_V(0);
    BAR;

#define READ_FRAGS(R, ua, sh, sb)                                                               \
    do {                                                                                        \
        const char* ap_ = smem + (ua) * AUNIT + aro[sh];                                        \
        const char* bp_ = smem + (sb) * BSLOT + bro;                                            \
        _Pragma("unroll") for (int m = 0; m < 4; ++m) ra[R][m] = *(const bf16x8*)(ap_ + m * 1024); \
        _Pragma("unroll") for (int n = 0; n < 4; ++n) rb[R][n] = *(const bf16x8*)(bp_ + n * 1024); \
    } while (0)
#define MMA(R)                                                                                  \
    do {                                                                                        \
        __builtin_amdgcn_s_setprio(1);                                                          \
        _Pragma("unroll") for (int m = 0; m < 4; ++m) _Pragma("unroll") for (int n = 0; n < 4; ++n) \
            acc[m][n] = __builtin_amdgcn_mfma_f32_16x16x32_bf16(rb[R][n], ra[R][m], acc[m][n], 0, 0, 0); \
        __builtin_amdgcn_s_setprio(0);                                                          \
    } while (0)

    READ_FRAGS(0, 0, 0, 0);

    int ue = 0;
    int sbn = 0;

#define PHASE(I, R, NV, LAST)                                                                   \
    do {                                                                                        \
        WAIT_L0;                                                                                \
        WAIT_V(NV);                                                                             \
        BAR;                                                                                    \
        if (!(LAST && I == 4)) {                                                                \
            const int un_ = (I == 4) ? (ue + 2) % 5 : ((I >= 2) ? (ue + 1) % 5 : ue);           \
            const int sh_ = (I == 4) ? 0 : ((I >= 2) ? I - 2 : I + 1);                          \
            const int sb_ = (sbn + 1) % 6;                                                      \
            if (!ABL_NOLDS) READ_FRAGS(R ^ 1, un_, sh_, sb_);                                                   \
        }                                                                                       \
        if (!(LAST)) {                                                                          \
            if (pf && !ABL_NOA) {                                                                           \
                if (I == 0 || I == 1)                                                           \
                    GLDS(xb + aoff[I] + (c + 2) * 64,                                           \
                         smem + ((ue + 4) % 5) * AUNIT + I * 8192 + wid * 1024 + lane * 16);    \
                if (I == 3 || I == 4)                                                           \
                    GLDS(xb + aoff[I - 1] + (c + 2) * 64,                                       \
                         smem + ue * AUNIT + (I - 3) * 8192 + wid * 1024 + lane * 16);          \
                if (I == 2 && wid < 2)                                                          \
                    GLDS(xb + aoffx + (c + 2) * 64,                                             \
                         smem + ((wid == 0) ? ((ue + 4) % 5) : ue) * AUNIT + 16384 + lane * 16); \
            }                                                                                   \
            if (!ABL_NOB) GLDS(wb + (c * 5 + I + 5) * BSLOT,                                                  \
                 smem + BOFF + ((sbn + 5) % 6) * BSLOT + wid * 1024 + lane * 16);               \
        }                                                                                       \
        MMA(R);                                                                                 \
        sbn = (sbn + 1) % 6;                                                                    \
    } while (0)

    for (int c = 0; c < 14; c += 2) {
        {
            const bool pf = true;
            PHASE(0, 0, 5, false); PHASE(1, 1, 6, false); PHASE(2, 0, 6, false); PHASE(3, 1, 5, false); PHASE(4, 0, 5, false);
            ue = (ue + 2) % 5;
        }
        {
            ++c;
            const bool pf = true;
            PHASE(0, 1, 5, false); PHASE(1, 0, 6, false); PHASE(2, 1, 6, false); PHASE(3, 0, 5, false); PHASE(4, 1, 5, false);
            ue = (ue + 2) % 5;
            --c;
        }
    }
    {
        const int c = 14;
        const bool pf = false;
        PHASE(0, 0, 3, false); PHASE(1, 1, 3, false); PHASE(2, 0, 3, false); PHASE(3, 1, 3, false); PHASE(4, 0, 3, false);
        ue = (ue + 2) % 5;
    }
    {
        const int c = 15;
        const bool pf = false;
        PHASE(0, 1, 3, true); PHASE(1, 0, 2, true); PHASE(2, 1, 1, true); PHASE(3, 0, 0, true); PHASE(4, 1, 0, true);
    }
    WAIT_L0;
    WAIT_V(0);
    BAR;

    if (ABL_NOEPI == 1) { float t=0; for(int m=0;m<4;++m)for(int n=0;n<4;++n)for(int r=0;r<4;++r)t+=acc[m][n][r]; if(t==1.2345f) out[0]=1; return; }
    float* red = (float*)smem;
    float* red2 = red + 512;
    float* csc = red2 + 512;
    float* csh = csc + 128;
    float* cmb = csh + 128;

    float bv[4][4];
#pragma unroll
    for (int n = 0; n < 4; ++n)
#pragma unroll
        for (int r = 0; r < 4; ++r)
            bv[n][r] = bf2f(bias[ntile * 128 + wn * 64 + (n >> 1) * 32 + 8 * fq + 4 * (n & 1) + r]);
    bool valid[4];
#pragma unroll
    for (int m = 0; m < 4; ++m) valid[m] = (l0 + wm * 64 + m * 16 + fr) < L_;
#pragma unroll
    for (int m = 0; m < 4; ++m)
#pragma unroll
        for (int n = 0; n < 4; ++n)
#pragma unroll
            for (int r = 0; r < 4; ++r) acc[m][n][r] += bv[n][r];

    const int cnt_rows = (L_ - l0) < BM ? (L_ - l0) : BM;
    float mean_t[4][4];
#pragma unroll
    for (int n = 0; n < 4; ++n)
#pragma unroll
        for (int r = 0; r < 4; ++r) {
            float s = 0.f;
#pragma unroll
            for (int m = 0; m < 4; ++m) s += valid[m] ? acc[m][n][r] : 0.f;
            s = row16_sum(s);
            if (fr == 0) red[wm * 128 + wn * 64 + (n >> 1) * 32 + 8 * fq + 4 * (n & 1) + r] = s;
        }
    __syncthreads();
    const float inv_cnt = 1.0f / (float)cnt_rows;
#pragma unroll
    for (int n = 0; n < 4; ++n)
#pragma unroll
        for (int r = 0; r < 4; ++r) {
            const int col = wn * 64 + (n >> 1) * 32 + 8 * fq + 4 * (n & 1) + r;
            mean_t[n][r] = (red[col] + red[128 + col] + red[256 + col] + red[384 + col]) * inv_cnt;
        }
#pragma unroll
    for (int n = 0; n < 4; ++n)
#pragma unroll
        for (int r = 0; r < 4; ++r) {
            float s = 0.f;
#pragma unroll
            for (int m = 0; m < 4; ++m) {
                const float d = acc[m][n][r] - mean_t[n][r];
                s += valid[m] ? d * d : 0.f;
            }
            s = row16_sum(s);
            if (fr == 0) red2[wm * 128 + wn * 64 + (n >> 1) * 32 + 8 * fq + 4 * (n & 1) + r] = s;
        }
    __syncthreads();
    if (tid < 128) {
        const float mt = (red[tid] + red[128 + tid] + red[256 + tid] + red[384 + tid]) * inv_cnt;
        const float m2 = red2[tid] + red2[128 + tid] + red2[256 + tid] + red2[384 + tid];
        const unsigned long long v = (unsigned long long)__float_as_uint(mt) | ((unsigned long long)__float_as_uint(m2) << 32);
        __hip_atomic_store((unsigned long long*)(stats + ((size_t)mtg * 512 + ntile * 128 + tid) * 2), v, __ATOMIC_RELAXED, __HIP_MEMORY_SCOPE_AGENT);
    }
    asm volatile("s_waitcnt vmcnt(0)" ::: "memory");
    __syncthreads();
    if (tid == 0) {
        unsigned* cp = cnt + (b * 4 + ntile) * 64;
        __hip_atomic_fetch_add(cp, 1u, __ATOMIC_RELAXED, __HIP_MEMORY_SCOPE_AGENT);
        unsigned spins = 0;
        while (__hip_atomic_load(cp, __ATOMIC_RELAXED, __HIP_MEMORY_SCOPE_AGENT) < (unsigned)(ABL_NOBAR ? 0 : MTB)) {
            __builtin_amdgcn_s_sleep(1);
            if (++spins > (1u << 24)) break;
        }
        __builtin_amdgcn_fence(__ATOMIC_ACQUIRE, "wavefront");
    }
    __syncthreads();
    {
        const int col = tid & 127, part = tid >> 7;
        float mi[8], m2i[8], ni[8];
        float s = 0.f;
#pragma unroll
        for (int k = 0; k < 8; ++k) {
            const int tt = part * 8 + k;
            const unsigned long long v = __hip_atomic_load((unsigned long long*)(stats + ((size_t)(b * 32 + tt) * 512 + ntile * 128 + col) * 2), __ATOMIC_RELAXED, __HIP_MEMORY_SCOPE_AGENT);
            mi[k] = __uint_as_float((unsigned)v); m2i[k] = __uint_as_float((unsigned)(v >> 32));
            ni[k] = (tt == 31) ? (float)(L_ - 31 * BM) : (float)BM;
            s += ni[k] * mi[k];
        }
        cmb[part * 128 + col] = s;
        __syncthreads();
        const float mean = (cmb[col] + cmb[128 + col] + cmb[256 + col] + cmb[384 + col]) * (1.0f / (float)L_);
        float q = 0.f;
#pragma unroll
        for (int k = 0; k < 8; ++k) { const float d = mi[k] - mean; q += m2i[k] + ni[k] * d * d; }
        __syncthreads();
        cmb[part * 128 + col] = q;
        __syncthreads();
        if (tid < 128) {
            const float var = (cmb[col] + cmb[128 + col] + cmb[256 + col] + cmb[384 + col]) * (1.0f / (float)L_);
            const float rstd = 1.0f / sqrtf(var + 1e-5f);
            const float gm = bf2f(gamma[ntile * 128 + col]), bt = bf2f(beta[ntile * 128 + col]);
            csc[col] = rstd * gm;
            csh[col] = bt - mean * rstd * gm;
        }
        __syncthreads();
    }
    if (ABL_NOEPI == 2) { if (csc[tid&127]==1.2345f) out[0]=1; return; }
    float sc[4][4], sh[4][4];
#pragma unroll
    for (int n = 0; n < 4; ++n)
#pragma unroll
        for (int r = 0; r < 4; ++r) {
            const int col = wn * 64 + (n >> 1) * 32 + 8 * fq + 4 * (n & 1) + r;
            sc[n][r] = csc[col]; sh[n][r] = csh[col];
        }
#pragma unroll
    for (int m = 0; m < 4; ++m) {
        const int l = l0 + wm * 64 + m * 16 + fr;
        if (l < L_) {
            unsigned short* orow = out + ((size_t)(b * L_ + l)) * 512 + ntile * 128 + wn * 64 + 8 * fq;
#pragma unroll
            for (int pr = 0; pr < 2; ++pr) {
                float v[8];
#pragma unroll
                for (int h = 0; h < 2; ++h)
#pragma unroll
                    for (int r = 0; r < 4; ++r)
                        v[h * 4 + r] = gelu_tanh(fmaf(acc[m][pr * 2 + h][r], sc[pr * 2 + h][r], sh[pr * 2 + h][r]));
                u32x4 o;
                o.x = pack_bf16(v[0], v[1]); o.y = pack_bf16(v[2], v[3]);
                o.z = pack_bf16(v[4], v[5]); o.w = pack_bf16(v[6], v[7]);
                *(u32x4*)(orow + pr * 32) = o;
            }
        }
    }
}

extern "C" void kernel_launch(void* const* d_in, const int* in_sizes, int n_in,
                              void* d_out, int out_size, void* d_ws, size_t ws_size,
                              hipStream_t stream) {
    const unsigned short* x = (const unsigned short*)d_in[0];
    const unsigned short* w = (const unsigned short*)d_in[1];
    const unsigned short* bias = (const unsigned short*)d_in[2];
    const unsigned short* gamma = (const unsigned short*)d_in[3];
    const unsigned short* beta = (const unsigned short*)d_in[4];
    char* ws = (char*)d_ws;
    unsigned short* wp = (unsigned short*)(ws + WS_WP);
    float* stats = (float*)(ws + WS_STATS);
    unsigned* cnt = (unsigned*)(ws + WS_CNT);
    (void)hipFuncSetAttribute((const void*)conv_gn_kernel, hipFuncAttributeMaxDynamicSharedMemorySize, LDS_BYTES);
    prep_kernel<<<4 * NPH, 256, 0, stream>>>(w, wp, cnt);
    conv_gn_kernel<<<256, 512, LDS_BYTES, stream>>>(x, wp, bias, gamma, beta, (unsigned short*)d_out, stats, cnt);
}
```
